```python
import torch

def _softmax(x, axis=-1):
    return torch.softmax(x, dim=axis)
def _gelu(x):
    return torch.nn.functional.gelu(x, approximate="tanh")
def _silu(x):
    return torch.nn.functional.silu(x)
def _sigmoid(x):
    return torch.sigmoid(x)
def _layernorm(x, axis=-1, eps=1e-5):
    m = x.mean(dim=axis, keepdim=True)
    v = x.var(dim=axis, keepdim=True, unbiased=False)
    return (x - m) / torch.sqrt(v + eps)
def _scatter_add(tgt, idx, src, axis=0):
    return tgt.clone().index_add_(axis, idx, src)
def _segment_sum(src, seg, n):
    out = torch.zeros((n,) + src.shape[1:], dtype=src.dtype)
    return out.index_add_(0, seg, src)
def _pad(x, pw):
    flat = tuple(v for pair in reversed(pw) for v in pair)
    return torch.nn.functional.pad(x, flat)

kernel_name = 'standalone__molecular_angular_gemnet__graph'

def setup_inputs():

    _w = (lambda a: (lambda t: t.to(torch.bfloat16) if t.is_floating_point() else t)(torch.as_tensor(a)))
    N, deg, De, L = 25000, 16, 4, 3
    E = N * deg
    T = E * deg
    NR = 8
    dist = torch.rand(E).to(float)
    angle = (torch.rand(T) * torch.pi).to(float)
    centers = torch.linspace(0.0, 1.0, NR).to(float)
    ang_freq = torch.arange(1, L + 1).to(float)
    return {
        "m": _w((lambda *s: torch.randn(*s, dtype=torch.bfloat16) * 0.05)(E, De)),
        "idx_in": _w((lambda lo, hi, *s: torch.randint(lo, hi, s))(0, E, T).to(torch.int64)),
        "idx_out": _w((lambda lo, hi, *s: torch.randint(lo, hi, s))(0, E, T).to(torch.int64)),
        "dist": _w(dist),
        "angle": _w(angle),
        "centers": _w(centers),
        "ang_freq": _w(ang_freq),
        "w_rbf": _w((lambda *s: torch.randn(*s, dtype=torch.bfloat16) * 0.4)(NR, De)),
        "w_sbf": _w((lambda *s: torch.randn(*s, dtype=torch.bfloat16) * 0.4)(NR * L, De)),
        "w_down": _w((lambda *s: torch.randn(*s, dtype=torch.bfloat16) * 0.4)(De, De)),
        "w_up": _w((lambda *s: torch.randn(*s, dtype=torch.bfloat16) * 0.4)(De, De)),
    }

def reference(m, idx_in, idx_out, dist, angle, centers, ang_freq, w_rbf, w_sbf, w_down, w_up):
    E, De = m.shape[0], m.shape[1]
    NR = centers.shape[0]
    L = ang_freq.shape[0]
    rbf = torch.exp(-10.0 * (dist[:, None] - centers[None, :]) ** 2)
    cbf = torch.cos(ang_freq[None, :] * angle[:, None])
    rbf_t = rbf[idx_out]
    sbf = torch.reshape(rbf_t[:, :, None] * cbf[:, None, :], (-1, NR * L))
    x = (m[idx_in] @ w_down) * (sbf @ w_sbf)
    agg = _segment_sum(x, idx_out, E)
    return m + (agg @ w_up) * (rbf @ w_rbf)
```

```cpp
#include <hip/hip_runtime.h>
#include <hip/hip_bf16.h>
#include <stdint.h>

__device__ __forceinline__ float bf2f(unsigned short v) { return __uint_as_float(((unsigned)v) << 16); }

__global__ void k_trip(const unsigned short* __restrict__ m, const int* __restrict__ idx_in, const int* __restrict__ idx_out,
                       const unsigned short* __restrict__ dist, const unsigned short* __restrict__ angle,
                       const unsigned short* __restrict__ centers, const unsigned short* __restrict__ freq,
                       const unsigned short* __restrict__ w_sbf, const unsigned short* __restrict__ w_down,
                       float* __restrict__ agg, int T) {
    int t = blockIdx.x * blockDim.x + threadIdx.x;
    if (t >= T) return;
    int i = idx_in[t], o = idx_out[t];
    float a = bf2f(angle[t]);
    float d = bf2f(dist[o]);
    float c[3];
    for (int l = 0; l < 3; ++l) c[l] = cosf(bf2f(freq[l]) * a);
    float s[4] = {0, 0, 0, 0};
    for (int r = 0; r < 8; ++r) {
        float df = d - bf2f(centers[r]);
        float rb = expf(-10.f * df * df);
        for (int l = 0; l < 3; ++l) {
            float sb = rb * c[l];
            for (int k = 0; k < 4; ++k) s[k] += sb * bf2f(w_sbf[(r * 3 + l) * 4 + k]);
        }
    }
    float mm[4];
    for (int k = 0; k < 4; ++k) mm[k] = bf2f(m[(size_t)i * 4 + k]);
    for (int k = 0; k < 4; ++k) {
        float md = 0;
        for (int j = 0; j < 4; ++j) md += mm[j] * bf2f(w_down[j * 4 + k]);
        atomicAdd(&agg[(size_t)o * 4 + k], md * s[k]);
    }
}

__global__ void k_fin(const unsigned short* __restrict__ m, const unsigned short* __restrict__ dist,
                      const unsigned short* __restrict__ centers, const unsigned short* __restrict__ w_rbf,
                      const unsigned short* __restrict__ w_up, const float* __restrict__ agg,
                      __hip_bfloat16* __restrict__ out, int E) {
    int e = blockIdx.x * blockDim.x + threadIdx.x;
    if (e >= E) return;
    float d = bf2f(dist[e]);
    float g[4] = {0, 0, 0, 0};
    for (int r = 0; r < 8; ++r) {
        float df = d - bf2f(centers[r]);
        float rb = expf(-10.f * df * df);
        for (int k = 0; k < 4; ++k) g[k] += rb * bf2f(w_rbf[r * 4 + k]);
    }
    for (int k = 0; k < 4; ++k) {
        float u = 0;
        for (int j = 0; j < 4; ++j) u += agg[(size_t)e * 4 + j] * bf2f(w_up[j * 4 + k]);
        out[(size_t)e * 4 + k] = __float2bfloat16(bf2f(m[(size_t)e * 4 + k]) + u * g[k]);
    }
}

extern "C" void kernel_launch(void* const* d_in, const int* in_sizes, int n_in,
                              void* d_out, int out_size, void* d_ws, size_t ws_size,
                              hipStream_t stream) {
    const unsigned short* m = (const unsigned short*)d_in[0];
    const int* idx_in = (const int*)d_in[1];
    const int* idx_out = (const int*)d_in[2];
    const unsigned short* dist = (const unsigned short*)d_in[3];
    const unsigned short* angle = (const unsigned short*)d_in[4];
    const unsigned short* centers = (const unsigned short*)d_in[5];
    const unsigned short* freq = (const unsigned short*)d_in[6];
    const unsigned short* w_rbf = (const unsigned short*)d_in[7];
    const unsigned short* w_sbf = (const unsigned short*)d_in[8];
    const unsigned short* w_down = (const unsigned short*)d_in[9];
    const unsigned short* w_up = (const unsigned short*)d_in[10];
    int E = in_sizes[3];
    int T = in_sizes[1];
    float* agg = (float*)d_ws;
    hipMemsetAsync(agg, 0, (size_t)E * 16, stream);
    k_trip<<<(T + 255) / 256, 256, 0, stream>>>(m, idx_in, idx_out, dist, angle, centers, freq, w_sbf, w_down, agg, T);
    k_fin<<<(E + 255) / 256, 256, 0, stream>>>(m, dist, centers, w_rbf, w_up, agg, (__hip_bfloat16*)d_out, E);
}
```

```cpp
#include <hip/hip_runtime.h>
#include <hip/hip_bf16.h>
#include <stdint.h>

typedef unsigned long long u64;
typedef unsigned short u16;
typedef __attribute__((ext_vector_type(2))) unsigned u32x2;
typedef __attribute__((ext_vector_type(3))) unsigned u32x3;
typedef __attribute__((ext_vector_type(4))) unsigned u32x4;
typedef __attribute__((ext_vector_type(4))) float f32x4;

#define NBKT 256
#define NCHUNK 256
#define K1_THREADS 1024
#define K1_QUADS 7
#define CHUNK_MAX 26624
#define MAXLOC 2048

__device__ __forceinline__ float bf2f(unsigned v) { return __uint_as_float(v << 16); }
__device__ __forceinline__ float bfhi(unsigned v) { return __uint_as_float(v & 0xffff0000u); }
__device__ __forceinline__ unsigned f2bf(float f) { return (unsigned)__builtin_bit_cast(u16, (__bf16)f); }
__device__ __forceinline__ float rbf16(float f) { return (float)(__bf16)f; }

__global__ void __launch_bounds__(K1_THREADS) k_part(const int* __restrict__ idx_in, const int* __restrict__ idx_out, const u16* __restrict__ angle,
                                                     const u16* __restrict__ m, const u16* __restrict__ w_down,
                                                     unsigned* __restrict__ recs, unsigned* __restrict__ tab, u64* __restrict__ md,
                                                     int T, int E, int chunk, int rec_stride  ) {
    extern __shared__ __attribute__((aligned(16))) unsigned smem[];
    unsigned* hist = smem;
    unsigned* base = smem + 256;
    unsigned* buf = smem + 512;
    const int tid = threadIdx.x;
    const int c = blockIdx.x;
    const int t0 = c * chunk;
    int n = T - t0; n = n < 0 ? 0 : (n > chunk ? chunk : n);
    if (tid < 256) hist[tid] = 0;
    __amdgpu_buffer_rsrc_t ro = __builtin_amdgcn_make_buffer_rsrc((void*)(idx_out + t0), 0, n * 4, 0x00020000);
    __amdgpu_buffer_rsrc_t ri = __builtin_amdgcn_make_buffer_rsrc((void*)(idx_in + t0), 0, n * 4, 0x00020000);
    __amdgpu_buffer_rsrc_t ra = __builtin_amdgcn_make_buffer_rsrc((void*)(angle + t0), 0, n * 2, 0x00020000);
    u32x4 vo[K1_QUADS], vi[K1_QUADS]; u32x2 va[K1_QUADS];
#pragma unroll
    for (int q = 0; q < K1_QUADS; ++q) vo[q] = __builtin_amdgcn_raw_buffer_load_b128(ro, (q * K1_THREADS + tid) * 16, 0, 2);
#pragma unroll
    for (int q = 0; q < K1_QUADS; ++q) {
        vi[q] = __builtin_amdgcn_raw_buffer_load_b128(ri, (q * K1_THREADS + tid) * 16, 0, 2);
        va[q] = __builtin_amdgcn_raw_buffer_load_b64(ra, (q * K1_THREADS + tid) * 8, 0, 2);
    }
    __syncthreads();
    unsigned rk[K1_QUADS][2];
#pragma unroll
    for (int q = 0; q < K1_QUADS; ++q) {
        unsigned r[4];
#pragma unroll
        for (int k = 0; k < 4; ++k) {
            unsigned b = (vo[q][k] >> 4) & 255u;
            r[k] = 0;
            if ((q * K1_THREADS + tid) * 4 + k < n) r[k] = __hip_atomic_fetch_add(&hist[b], 1u, __ATOMIC_RELAXED, __HIP_MEMORY_SCOPE_WORKGROUP);
        }
        rk[q][0] = r[0] | (r[1] << 16); rk[q][1] = r[2] | (r[3] << 16);
        asm volatile("" : "+v"(rk[q][0]), "+v"(rk[q][1]));
    }
    __syncthreads();
    if (tid < 64) {
        u32x4 h = *(u32x4*)&hist[tid * 4];
        unsigned s = h[0] + h[1] + h[2] + h[3];
        unsigned inc = s;
#pragma unroll
        for (int d = 1; d < 64; d <<= 1) { unsigned y = __shfl_up(inc, d); if (tid >= d) inc += y; }
        unsigned ex = inc - s;
        u32x4 bs; bs[0] = ex; bs[1] = ex + h[0]; bs[2] = bs[1] + h[1]; bs[3] = bs[2] + h[2];
        *(u32x4*)&base[tid * 4] = bs;
        u32x4 tv; tv[0] = bs[0] | (h[0] << 16); tv[1] = bs[1] | (h[1] << 16); tv[2] = bs[2] | (h[2] << 16); tv[3] = bs[3] | (h[3] << 16);
        *(u32x4*)&tab[c * NBKT + tid * 4] = tv;
    }
    __syncthreads();
#pragma unroll
    for (int q = 0; q < K1_QUADS; ++q) {
#pragma unroll
        for (int k = 0; k < 4; ++k) {
            if ((q * K1_THREADS + tid) * 4 + k < n) {
                unsigned o = vo[q][k];
                asm volatile("" : "+v"(o));
                unsigned pos = base[(o >> 4) & 255u] + ((k & 1) ? (rk[q][k >> 1] >> 16) : (rk[q][k >> 1] & 0xffffu));
                unsigned pr = (pos >> 1) * 3, odd = pos & 1u;
                buf[pr + odd] = ((((o >> 12) << 4) | (o & 15u)) << 19) | vi[q][k];
                unsigned a = va[q][k >> 1]; a = (k & 1) ? (a >> 16) : a;
                ((u16*)(buf + pr + 2))[odd] = (u16)a;
            }
        }
    }
    {
        int per = (E + gridDim.x - 1) / gridDim.x;
        float wd[16];
#pragma unroll
        for (int i = 0; i < 16; ++i) wd[i] = bf2f(w_down[i]);
        for (int e = c * per + tid; e < (c + 1) * per && e < E; e += K1_THREADS) {
            u32x2 mv = *(const u32x2*)(m + (size_t)e * 4);
            float m0 = bf2f(mv[0]), m1 = bfhi(mv[0]), m2 = bf2f(mv[1]), m3 = bfhi(mv[1]);
            unsigned r[4];
#pragma unroll
            for (int d = 0; d < 4; ++d) r[d] = f2bf(m0 * wd[d] + m1 * wd[4 + d] + m2 * wd[8 + d] + m3 * wd[12 + d]);
            md[e] = (u64)(r[0] | (r[1] << 16)) | ((u64)(r[2] | (r[3] << 16)) << 32);
        }
    }
    __syncthreads();
    int nq = ((n + 1) / 2 * 3 + 3) / 4;
    u32x4* dst = (u32x4*)(recs + (size_t)c * rec_stride);
    for (int i = tid; i < nq; i += K1_THREADS) __builtin_nontemporal_store(*(u32x4*)&buf[i * 4], dst + i);
}

__global__ void __launch_bounds__(1024) k_acc(const unsigned* __restrict__ recs, const unsigned* __restrict__ tab, const u64* __restrict__ md,
                                              const u16* __restrict__ m, const u16* __restrict__ dist, const u16* __restrict__ centers, const u16* __restrict__ freq,
                                              const u16* __restrict__ w_rbf, const u16* __restrict__ w_sbf, const u16* __restrict__ w_up,
                                              u16* __restrict__ out, int E, int nchunk, int rec_stride, int nloc) {
    extern __shared__ __attribute__((aligned(16))) unsigned smem[];
    double* acc = (double*)smem;
    f32x4* G = (f32x4*)(acc + 4 * nloc);
    unsigned* seg = (unsigned*)(G + 3 * nloc);
    const int tid = threadIdx.x, lane = tid & 63, wave = __builtin_amdgcn_readfirstlane(tid >> 6);
    const int b = blockIdx.x;
    for (int c = tid; c < nchunk; c += 1024) seg[c] = tab[c * NBKT + b];
    float gate[2][4];
#pragma unroll
    for (int j = 0; j < 2; ++j) {
        int l = tid + j * 1024;
        int e = ((l >> 4) << 12) | (b << 4) | (l & 15);
        float g[12];
#pragma unroll
        for (int i = 0; i < 12; ++i) g[i] = 0.f;
#pragma unroll
        for (int d = 0; d < 4; ++d) gate[j][d] = 0.f;
        if (l < nloc) {
            float dd = e < E ? bf2f(dist[e]) : 0.f;
#pragma unroll
            for (int r = 0; r < 8; ++r) {
                float df = rbf16(dd - bf2f(centers[r]));
                float sq = rbf16(df * df);
                float ar = rbf16(-10.f * sq);
                float rb = rbf16(__expf(ar));
#pragma unroll
                for (int i = 0; i < 12; ++i) g[i] += rb * bf2f(w_sbf[r * 12 + i]);
#pragma unroll
                for (int d = 0; d < 4; ++d) gate[j][d] += rb * bf2f(w_rbf[r * 4 + d]);
            }
#pragma unroll
            for (int k = 0; k < 3; ++k) G[k * nloc + l] = (f32x4){g[k * 4], g[k * 4 + 1], g[k * 4 + 2], g[k * 4 + 3]};
#pragma unroll
            for (int d = 0; d < 4; ++d) acc[d * nloc + l] = 0.0;
        }
    }
    const float f0 = bf2f(freq[0]), f1 = bf2f(freq[1]), f2 = bf2f(freq[2]);
    const float inv2pi = 0.15915494309189535f;
    __syncthreads();
    for (int c = wave; c < nchunk; c += 16) {
        unsigned sv = __builtin_amdgcn_readfirstlane(seg[c]);
        int start = sv & 0xffffu, cnt = sv >> 16, end = start + cnt;
        const unsigned* src = recs + (size_t)c * rec_stride;
        for (int p0 = start >> 1; p0 * 2 < end; p0 += 64) {
            int p = p0 + lane;
            bool v0 = (2 * p >= start) && (2 * p < end), v1 = (2 * p + 1 < end);
            u32x3 r = (u32x3){0, 0, 0};
            if (v0 || v1) r = __builtin_nontemporal_load((const u32x3*)(src + p * 3));
            u64 g0 = 0, g1 = 0;
            if (v0) g0 = md[r[0] & 0x7ffffu];
            if (v1) g1 = md[r[1] & 0x7ffffu];
#pragma unroll
            for (int h = 0; h < 2; ++h) {
                if (h ? v1 : v0) {
                    unsigned w = r[h]; u64 g = h ? g1 : g0;
                    float a = h ? bfhi(r[2]) : bf2f(r[2]);
                    unsigned l = w >> 19;
                    float c0 = __builtin_amdgcn_cosf(__builtin_amdgcn_fractf(rbf16(f0 * a) * inv2pi));
                    float c1 = __builtin_amdgcn_cosf(__builtin_amdgcn_fractf(rbf16(f1 * a) * inv2pi));
                    float c2 = __builtin_amdgcn_cosf(__builtin_amdgcn_fractf(rbf16(f2 * a) * inv2pi));
                    f32x4 s = G[l] * c0 + G[nloc + l] * c1 + G[2 * nloc + l] * c2;
                    unsigned lo = (unsigned)g, hi = (unsigned)(g >> 32);
                    float x0 = bf2f(lo) * s[0], x1 = bfhi(lo) * s[1], x2 = bf2f(hi) * s[2], x3 = bfhi(hi) * s[3];
                    __hip_atomic_fetch_add(&acc[l], (double)x0, __ATOMIC_RELAXED, __HIP_MEMORY_SCOPE_WORKGROUP);
                    __hip_atomic_fetch_add(&acc[nloc + l], (double)x1, __ATOMIC_RELAXED, __HIP_MEMORY_SCOPE_WORKGROUP);
                    __hip_atomic_fetch_add(&acc[2 * nloc + l], (double)x2, __ATOMIC_RELAXED, __HIP_MEMORY_SCOPE_WORKGROUP);
                    __hip_atomic_fetch_add(&acc[3 * nloc + l], (double)x3, __ATOMIC_RELAXED, __HIP_MEMORY_SCOPE_WORKGROUP);
                }
            }
        }
    }
    __syncthreads();
    float wu[16];
#pragma unroll
    for (int i = 0; i < 16; ++i) wu[i] = bf2f(w_up[i]);
#pragma unroll
    for (int j = 0; j < 2; ++j) {
        int l = tid + j * 1024;
        int e = ((l >> 4) << 12) | (b << 4) | (l & 15);
        if (l < nloc && e < E) {
            float a0 = (float)acc[l], a1 = (float)acc[nloc + l], a2 = (float)acc[2 * nloc + l], a3 = (float)acc[3 * nloc + l];
            u32x2 mv = *(const u32x2*)(m + (size_t)e * 4);
            float mm[4] = {bf2f(mv[0]), bfhi(mv[0]), bf2f(mv[1]), bfhi(mv[1])};
            unsigned r[4];
#pragma unroll
            for (int d = 0; d < 4; ++d) {
                float u = a0 * wu[d] + a1 * wu[4 + d] + a2 * wu[8 + d] + a3 * wu[12 + d];
                r[d] = f2bf(mm[d] + u * gate[j][d]);
            }
            *(u32x2*)(out + (size_t)e * 4) = (u32x2){r[0] | (r[1] << 16), r[2] | (r[3] << 16)};
        }
    }
}

extern "C" void kernel_launch(void* const* d_in, const int* in_sizes, int n_in,
                              void* d_out, int out_size, void* d_ws, size_t ws_size,
                              hipStream_t stream) {
    const u16* m = (const u16*)d_in[0];
    const int* idx_in = (const int*)d_in[1];
    const int* idx_out = (const int*)d_in[2];
    const u16* dist = (const u16*)d_in[3];
    const u16* angle = (const u16*)d_in[4];
    const u16* centers = (const u16*)d_in[5];
    const u16* freq = (const u16*)d_in[6];
    const u16* w_rbf = (const u16*)d_in[7];
    const u16* w_sbf = (const u16*)d_in[8];
    const u16* w_down = (const u16*)d_in[9];
    const u16* w_up = (const u16*)d_in[10];
    int E = in_sizes[3];
    int T = in_sizes[1];
    int chunk = ((T + NCHUNK - 1) / NCHUNK + 7) / 8 * 8;
    int nchunk = NCHUNK;
    if (chunk > CHUNK_MAX) { chunk = CHUNK_MAX; nchunk = (T + chunk - 1) / chunk; }
    int rec_stride = ((chunk / 2 * 3) + 31) / 32 * 32;
    int nloc = ((E + 4095) / 4096) * 16;
    char* ws = (char*)d_ws;
    u64* md = (u64*)ws;
    size_t off = ((size_t)E * 8 + 255) / 256 * 256;
    unsigned* tab = (unsigned*)(ws + off); off += (size_t)nchunk * NBKT * 4;
    unsigned* recs = (unsigned*)(ws + off);
    size_t lds1 = 2048 + (size_t)(chunk / 2 * 3 + 4) * 4;
    size_t lds2 = (size_t)nloc * (32 + 48) + (size_t)nchunk * 4;
    (void)hipFuncSetAttribute((const void*)k_part, hipFuncAttributeMaxDynamicSharedMemorySize, 160 * 1024);
    (void)hipFuncSetAttribute((const void*)k_acc, hipFuncAttributeMaxDynamicSharedMemorySize, 160 * 1024);
    k_part<<<nchunk, K1_THREADS, lds1, stream>>>(idx_in, idx_out, angle, m, w_down, recs, tab, md, T, E, chunk, rec_stride);
    k_acc<<<NBKT, 1024, lds2, stream>>>(recs, tab, md, m, dist, centers, freq, w_rbf, w_sbf, w_up, (u16*)d_out, E, nchunk, rec_stride, nloc);
}
```

```cpp
#include <hip/hip_runtime.h>
#include <hip/hip_bf16.h>
#include <stdint.h>

typedef unsigned long long u64;
typedef unsigned short u16;
typedef __attribute__((ext_vector_type(2))) unsigned u32x2;
typedef __attribute__((ext_vector_type(3))) unsigned u32x3;
typedef __attribute__((ext_vector_type(4))) unsigned u32x4;
typedef __attribute__((ext_vector_type(4))) float f32x4;

#define NBKT 256
#define NCHUNK 256
#define K1_THREADS 1024
#define K1_QUADS 7
#define CHUNK_MAX 26624
#define SB() __builtin_amdgcn_sched_barrier(0)
#define STAMP(k, i)
#define STAMPW(k, i)

__device__ __forceinline__ float bf2f(unsigned v) { return __uint_as_float(v << 16); }
__device__ __forceinline__ float bfhi(unsigned v) { return __uint_as_float(v & 0xffff0000u); }
__device__ __forceinline__ unsigned f2bf(float f) { return (unsigned)__builtin_bit_cast(u16, (__bf16)f); }
__device__ __forceinline__ float rbf16(float f) { return (float)(__bf16)f; }
__device__ __forceinline__ float wld(const u16* p, int i) { unsigned v = ((const unsigned*)p)[i >> 1]; return (i & 1) ? bfhi(v) : bf2f(v); }
__device__ __forceinline__ u64 gat(const u64* md, unsigned w) { return *(const u64*)((const char*)md + ((w & 0x7ffffu) << 3)); }

__global__ void __launch_bounds__(K1_THREADS) k_part(const int* __restrict__ idx_in, const int* __restrict__ idx_out, const u16* __restrict__ angle,
                                                     const u16* __restrict__ m, const u16* __restrict__ w_down,
                                                     unsigned* __restrict__ recs, unsigned* __restrict__ tab, u64* __restrict__ md,
                                                     int T, int E, int chunk, int rec_stride  , int eper) {
    extern __shared__ __attribute__((aligned(16))) unsigned smem[];
    unsigned* hist = smem;
    unsigned* buf = smem + 256;
    const int tid = threadIdx.x;
    const int c = blockIdx.x;
    const int t0 = c * chunk;
    int n = T - t0; n = n < 0 ? 0 : (n > chunk ? chunk : n);
    const int nq4 = n >> 2;
    STAMP(0, 0);
    if (tid < 256) hist[tid] = 0;
    __amdgpu_buffer_rsrc_t ro = __builtin_amdgcn_make_buffer_rsrc((void*)(idx_out + t0), 0, n * 4, 0x00020000);
    __amdgpu_buffer_rsrc_t ri = __builtin_amdgcn_make_buffer_rsrc((void*)(idx_in + t0), 0, n * 4, 0x00020000);
    __amdgpu_buffer_rsrc_t ra = __builtin_amdgcn_make_buffer_rsrc((void*)(angle + t0), 0, n * 2, 0x00020000);
    u32x4 vo[K1_QUADS], vi[K1_QUADS]; u32x2 va[K1_QUADS];
#pragma unroll
    for (int q = 0; q < K1_QUADS; ++q) { vo[q] = __builtin_amdgcn_raw_buffer_load_b128(ro, (q * K1_THREADS + tid) * 16, 0, 2); SB(); }
    u32x2 mv[2];
    {
        __amdgpu_buffer_rsrc_t rm = __builtin_amdgcn_make_buffer_rsrc((void*)m, 0, E * 8, 0x00020000);
#pragma unroll
        for (int j = 0; j < 2; ++j) mv[j] = __builtin_amdgcn_raw_buffer_load_b64(rm, (c * eper + j * K1_THREADS + tid) * 8, 0, 0);
        SB();
    }
#pragma unroll
    for (int q = 0; q < K1_QUADS; ++q) {
        vi[q] = __builtin_amdgcn_raw_buffer_load_b128(ri, (q * K1_THREADS + tid) * 16, 0, 2);
        va[q] = __builtin_amdgcn_raw_buffer_load_b64(ra, (q * K1_THREADS + tid) * 8, 0, 2);
        SB();
    }
    __syncthreads();
#pragma unroll
    for (int q = 0; q < K1_QUADS; ++q) {
        if (q * K1_THREADS + tid < nq4) {
#pragma unroll
            for (int k = 0; k < 4; ++k) __hip_atomic_fetch_add(&hist[(vo[q][k] >> 4) & 255u], 1u, __ATOMIC_RELAXED, __HIP_MEMORY_SCOPE_WORKGROUP);
        }
        SB();
    }
    __syncthreads();
    STAMP(0, 1);
    if (tid < 64) {
        u32x4 h = *(u32x4*)&hist[tid * 4];
        unsigned s = h[0] + h[1] + h[2] + h[3];
        unsigned inc = s;
#pragma unroll
        for (int d = 1; d < 64; d <<= 1) { unsigned y = __shfl_up(inc, d); if (tid >= d) inc += y; }
        unsigned ex = inc - s;
        u32x4 bs; bs[0] = ex; bs[1] = ex + h[0]; bs[2] = bs[1] + h[1]; bs[3] = bs[2] + h[2];
        *(u32x4*)&hist[tid * 4] = bs;
        u32x4 tv; tv[0] = bs[0] | (h[0] << 16); tv[1] = bs[1] | (h[1] << 16); tv[2] = bs[2] | (h[2] << 16); tv[3] = bs[3] | (h[3] << 16);
        *(u32x4*)&tab[c * NBKT + tid * 4] = tv;
    }
    {
        float wd[16];
#pragma unroll
        for (int i = 0; i < 16; ++i) wd[i] = wld(w_down, i);
#pragma unroll
        for (int j = 0; j < 2; ++j) {
            int le = j * K1_THREADS + tid, e = c * eper + le;
            float m0 = bf2f(mv[j][0]), m1 = bfhi(mv[j][0]), m2 = bf2f(mv[j][1]), m3 = bfhi(mv[j][1]);
            unsigned r[4];
#pragma unroll
            for (int d = 0; d < 4; ++d) r[d] = f2bf(m0 * wd[d] + m1 * wd[4 + d] + m2 * wd[8 + d] + m3 * wd[12 + d]);
            if (le < eper && e < E) md[e] = (u64)(r[0] | (r[1] << 16)) | ((u64)(r[2] | (r[3] << 16)) << 32);
        }
    }
    __syncthreads();
    STAMP(0, 2);
#pragma unroll
    for (int q = 0; q < K1_QUADS; ++q) {
        if (q * K1_THREADS + tid < nq4) {
            unsigned pos[4];
#pragma unroll
            for (int k = 0; k < 4; ++k) {
                unsigned o = vo[q][k];
                asm volatile("" : "+v"(o));
                pos[k] = __hip_atomic_fetch_add(&hist[(o >> 4) & 255u], 1u, __ATOMIC_RELAXED, __HIP_MEMORY_SCOPE_WORKGROUP);
                vo[q][k] = (((o >> 12) << 4) | (o & 15u)) << 19;
            }
#pragma unroll
            for (int k = 0; k < 4; ++k) vo[q][k] |= pos[k];
        }
        SB();
    }
#pragma unroll
    for (int q = 0; q < K1_QUADS; ++q) {
        if (q * K1_THREADS + tid < nq4) {
#pragma unroll
            for (int k = 0; k < 4; ++k) {
                unsigned pos = vo[q][k] & 0x7ffffu;
                unsigned pr = (pos >> 1) * 3, odd = pos & 1u;
                buf[pr + odd] = (vo[q][k] & 0xfff80000u) | vi[q][k];
                unsigned a = va[q][k >> 1]; a = (k & 1) ? (a >> 16) : a;
                ((u16*)(buf + pr + 2))[odd] = (u16)a;
            }
        }
        SB();
    }
    STAMP(0, 3);
    __syncthreads();
    STAMP(0, 4);
    int nq = ((n + 1) / 2 * 3 + 3) / 4;
    u32x4* dst = (u32x4*)(recs + (size_t)c * rec_stride);
    for (int i = tid; i < nq; i += K1_THREADS) __builtin_nontemporal_store(*(u32x4*)&buf[i * 4], dst + i);
    STAMP(0, 5);
    STAMP(0, 6);
}

struct Ctx { double* acc; const f32x4* G; int nloc; float f0, f1, f2; };

__device__ __forceinline__ void accum(const Ctx& x, unsigned w, float a, u64 g) {
    const float inv2pi = 0.15915494309189535f;
    unsigned l = w >> 19;
    float c0 = __builtin_amdgcn_cosf(__builtin_amdgcn_fractf(rbf16(x.f0 * a) * inv2pi));
    float c1 = __builtin_amdgcn_cosf(__builtin_amdgcn_fractf(rbf16(x.f1 * a) * inv2pi));
    float c2 = __builtin_amdgcn_cosf(__builtin_amdgcn_fractf(rbf16(x.f2 * a) * inv2pi));
    f32x4 s = x.G[l] * c0 + x.G[x.nloc + l] * c1 + x.G[2 * x.nloc + l] * c2;
    unsigned lo = (unsigned)g, hi = (unsigned)(g >> 32);
    float x0 = bf2f(lo) * s[0], x1 = bfhi(lo) * s[1], x2 = bf2f(hi) * s[2], x3 = bfhi(hi) * s[3];
    __hip_atomic_fetch_add(&x.acc[l], (double)x0, __ATOMIC_RELAXED, __HIP_MEMORY_SCOPE_WORKGROUP);
    __hip_atomic_fetch_add(&x.acc[x.nloc + l], (double)x1, __ATOMIC_RELAXED, __HIP_MEMORY_SCOPE_WORKGROUP);
    __hip_atomic_fetch_add(&x.acc[2 * x.nloc + l], (double)x2, __ATOMIC_RELAXED, __HIP_MEMORY_SCOPE_WORKGROUP);
    __hip_atomic_fetch_add(&x.acc[3 * x.nloc + l], (double)x3, __ATOMIC_RELAXED, __HIP_MEMORY_SCOPE_WORKGROUP);
}

#define NSEG 16
#define GDEPTH 4
__global__ void __launch_bounds__(1024) k_acc(const unsigned* __restrict__ recs, const unsigned* __restrict__ tab, const u64* __restrict__ md,
                                              const u16* __restrict__ m, const u16* __restrict__ dist, const u16* __restrict__ centers, const u16* __restrict__ freq,
                                              const u16* __restrict__ w_rbf, const u16* __restrict__ w_sbf, const u16* __restrict__ w_up,
                                              u16* __restrict__ out, int E, int nchunk, int rec_stride, int nloc) {
    extern __shared__ __attribute__((aligned(16))) unsigned smem[];
    double* acc = (double*)smem;
    f32x4* G = (f32x4*)(acc + 4 * nloc);
    const int tid = threadIdx.x, lane = tid & 63, wave = __builtin_amdgcn_readfirstlane(tid >> 6);
    const int b = blockIdx.x;
    STAMP(1, 0);
    Ctx x; x.acc = acc; x.G = G; x.nloc = nloc; x.f0 = wld(freq, 0); x.f1 = wld(freq, 1); x.f2 = bf2f(__builtin_amdgcn_readfirstlane((unsigned)freq[2]));
    float gate[2][4];
    for (int cg = 0; cg < nchunk; cg += 16 * NSEG) {
        unsigned sv = 0;
        { int c = cg + wave + 16 * lane; if (lane < NSEG && c < nchunk) sv = tab[c * NBKT + b]; }
        float dd[2];
        if (cg == 0) {
#pragma unroll
            for (int j = 0; j < 2; ++j) {
                int l = tid + j * 1024;
                int e = ((l >> 4) << 12) | (b << 4) | (l & 15);
                dd[j] = (l < nloc && e < E) ? bf2f(dist[e]) : 0.f;
            }
        }
        SB();
        STAMP(1, 1);
        u32x3 r[NSEG];
#pragma unroll
        for (int i = 0; i < NSEG; ++i) {
            unsigned s = __builtin_amdgcn_readlane(sv, i);
            int start = s & 0xffffu, end = start + (s >> 16);
            int p0 = start >> 1;
            int nby = (((end + 1) >> 1) - p0) * 12;
            const unsigned* src = recs + (size_t)(cg + wave + 16 * i) * rec_stride + p0 * 3;
            __amdgpu_buffer_rsrc_t rs = __builtin_amdgcn_make_buffer_rsrc((void*)src, 0, nby, 0x00020000);
            r[i] = __builtin_amdgcn_raw_buffer_load_b96(rs, lane * 12, 0, 2);
            SB();
        }
        if (cg == 0) {
            float g[2][12];
#pragma unroll
            for (int j = 0; j < 2; ++j) {
#pragma unroll
                for (int i = 0; i < 12; ++i) g[j][i] = 0.f;
#pragma unroll
                for (int d = 0; d < 4; ++d) gate[j][d] = 0.f;
            }
#pragma unroll 1
            for (int rr = 0; rr < 8; ++rr) {
                float cen = wld(centers, rr);
                const unsigned* ws = (const unsigned*)w_sbf + rr * 6;
                const unsigned* wr = (const unsigned*)w_rbf + rr * 2;
#pragma unroll
                for (int j = 0; j < 2; ++j) {
                    float df = rbf16(dd[j] - cen);
                    float sq = rbf16(df * df);
                    float ar = rbf16(-10.f * sq);
                    float rb = rbf16(__expf(ar));
#pragma unroll
                    for (int i = 0; i < 6; ++i) { g[j][2 * i] += rb * bf2f(ws[i]); g[j][2 * i + 1] += rb * bfhi(ws[i]); }
#pragma unroll
                    for (int d = 0; d < 2; ++d) { gate[j][2 * d] += rb * bf2f(wr[d]); gate[j][2 * d + 1] += rb * bfhi(wr[d]); }
                }
            }
#pragma unroll
            for (int j = 0; j < 2; ++j) {
                int l = tid + j * 1024;
                if (l < nloc) {
#pragma unroll
                    for (int k = 0; k < 3; ++k) G[k * nloc + l] = (f32x4){g[j][k * 4], g[j][k * 4 + 1], g[j][k * 4 + 2], g[j][k * 4 + 3]};
#pragma unroll
                    for (int d = 0; d < 4; ++d) acc[d * nloc + l] = 0.0;
                }
            }
            STAMP(1, 2);
            __syncthreads();
        }
        STAMP(1, 3);
        SB();
        u64 g0[NSEG], g1[NSEG];
#pragma unroll
        for (int i = 0; i < NSEG + GDEPTH; ++i) {
            if (i < NSEG) {
                g0[i] = gat(md, r[i][0]);
                g1[i] = gat(md, r[i][1]);
            }
            SB();
            if (i == GDEPTH + 1) STAMP(1, 4);
            if (i == GDEPTH + 8) STAMP(1, 5);
            if (i >= GDEPTH) {
                int k = i - GDEPTH;
                unsigned s = __builtin_amdgcn_readlane(sv, k);
                int start = s & 0xffffu, end = start + (s >> 16);
                int p2 = (start & ~1) + 2 * lane;
                if (p2 >= start && p2 < end) accum(x, r[k][0], bf2f(r[k][2]), g0[k]);
                if (p2 + 1 < end) accum(x, r[k][1], bfhi(r[k][2]), g1[k]);
            }
            SB();
        }
        STAMP(1, 6);
#pragma unroll 1
        for (int i = 0; i < NSEG; ++i) {
            unsigned s = __builtin_amdgcn_readlane(sv, i);
            int start = s & 0xffffu, end = start + (s >> 16);
            const unsigned* src = recs + (size_t)(cg + wave + 16 * i) * rec_stride;
            for (int p0 = (start >> 1) + 64; p0 * 2 < end; p0 += 64) {
                int p = p0 + lane;
                if (2 * p < end) {
                    u32x3 rr = *(const u32x3*)(src + p * 3);
                    accum(x, rr[0], bf2f(rr[2]), gat(md, rr[0]));
                    if (2 * p + 1 < end) accum(x, rr[1], bfhi(rr[2]), gat(md, rr[1]));
                }
            }
        }
    }
    __syncthreads();
    STAMP(1, 7);
    float wu[16];
#pragma unroll
    for (int i = 0; i < 16; ++i) wu[i] = wld(w_up, i);
#pragma unroll
    for (int j = 0; j < 2; ++j) {
        int l = tid + j * 1024;
        int e = ((l >> 4) << 12) | (b << 4) | (l & 15);
        if (l < nloc && e < E) {
            float a0 = (float)acc[l], a1 = (float)acc[nloc + l], a2 = (float)acc[2 * nloc + l], a3 = (float)acc[3 * nloc + l];
            u32x2 mv = *(const u32x2*)(m + (size_t)e * 4);
            float mm[4] = {bf2f(mv[0]), bfhi(mv[0]), bf2f(mv[1]), bfhi(mv[1])};
            unsigned r[4];
#pragma unroll
            for (int d = 0; d < 4; ++d) {
                float u = a0 * wu[d] + a1 * wu[4 + d] + a2 * wu[8 + d] + a3 * wu[12 + d];
                r[d] = f2bf(mm[d] + u * gate[j][d]);
            }
            *(u32x2*)(out + (size_t)e * 4) = (u32x2){r[0] | (r[1] << 16), r[2] | (r[3] << 16)};
        }
    }
    STAMP(1, 8);
}

extern "C" void kernel_launch(void* const* d_in, const int* in_sizes, int n_in,
                              void* d_out, int out_size, void* d_ws, size_t ws_size,
                              hipStream_t stream) {
    const u16* m = (const u16*)d_in[0];
    const int* idx_in = (const int*)d_in[1];
    const int* idx_out = (const int*)d_in[2];
    const u16* dist = (const u16*)d_in[3];
    const u16* angle = (const u16*)d_in[4];
    const u16* centers = (const u16*)d_in[5];
    const u16* freq = (const u16*)d_in[6];
    const u16* w_rbf = (const u16*)d_in[7];
    const u16* w_sbf = (const u16*)d_in[8];
    const u16* w_down = (const u16*)d_in[9];
    const u16* w_up = (const u16*)d_in[10];
    int E = in_sizes[3];
    int T = in_sizes[1];
    int chunk = ((T + NCHUNK - 1) / NCHUNK + 7) / 8 * 8;
    int nchunk = NCHUNK;
    if (chunk > CHUNK_MAX) { chunk = CHUNK_MAX; nchunk = (T + chunk - 1) / chunk; }
    int rec_stride = ((chunk / 2 * 3) + 31) / 32 * 32;
    int nloc = ((E + 4095) / 4096) * 16;
    int eper = (E + nchunk - 1) / nchunk;
    char* ws = (char*)d_ws;
    u64* md = (u64*)ws;
    size_t off = (size_t)524288 * 8;
    unsigned* tab = (unsigned*)(ws + off); off += (size_t)nchunk * NBKT * 4;
    unsigned* recs = (unsigned*)(ws + off);
    size_t lds1 = 1024 + (size_t)(chunk / 2 * 3 + 4) * 4;
    size_t lds2 = (size_t)nloc * (32 + 48);
    (void)hipFuncSetAttribute((const void*)k_part, hipFuncAttributeMaxDynamicSharedMemorySize, 160 * 1024);
    (void)hipFuncSetAttribute((const void*)k_acc, hipFuncAttributeMaxDynamicSharedMemorySize, 160 * 1024);
    k_part<<<nchunk, K1_THREADS, lds1, stream>>>(idx_in, idx_out, angle, m, w_down, recs, tab, md, T, E, chunk, rec_stride, eper);
    k_acc<<<NBKT, 1024, lds2, stream>>>(recs, tab, md, m, dist, centers, freq, w_rbf, w_sbf, w_up, (u16*)d_out, E, nchunk, rec_stride, nloc);
}
```

```cpp
#include <hip/hip_runtime.h>
#include <hip/hip_bf16.h>
#include <stdint.h>
#pragma clang diagnostic ignored "-Wint-to-pointer-cast"

typedef unsigned long long u64;
typedef unsigned short u16;
typedef unsigned char u8;
typedef __attribute__((ext_vector_type(2))) unsigned u32x2;
typedef __attribute__((ext_vector_type(3))) unsigned u32x3;
typedef __attribute__((ext_vector_type(4))) unsigned u32x4;
typedef __attribute__((ext_vector_type(8))) unsigned u32x8;
typedef __attribute__((ext_vector_type(2))) __bf16 bf16x2;
typedef __attribute__((ext_vector_type(8))) __bf16 bf16x8;
typedef __attribute__((ext_vector_type(16))) float f32x16;
#define GAS __attribute__((address_space(1)))
#define LAS __attribute__((address_space(3)))
#define RLX_AGENT __ATOMIC_RELAXED, __HIP_MEMORY_SCOPE_AGENT
#define RLX_WG __ATOMIC_RELAXED, __HIP_MEMORY_SCOPE_WORKGROUP
#define LP(T, a) ((LAS T*)(unsigned)(a))

#define NWG 256
#define NT 1024
#define QUADS 7
#define AHEAD 2
#define NS1 6
#define GD 2
#define LDS_BYTES 163840
#define L1_H 128
#define L1_BS 1152
#define L1_BUF 2192
#define L_CTL 16
#define C_OVF (L_CTL + 0)
#define C_FAIL (L_CTL + 4)
#define C_GMAX (L_CTL + 8)
#define C_GMIN (L_CTL + 12)
#define C_WE (L_CTL + 32)
#define C_WD (L_CTL + 48)
#define C_ARR (L_CTL + 16)
#define C_MAXE (L_CTL + 20)
#define C_MINE (L_CTL + 24)
#define CHUNK_MAX 26488
static_assert(L1_BUF + 6 * (CHUNK_MAX + 256) + 16 <= LDS_BYTES, "LDS map");
#define NST 10
#define NLOC 1600
#define CT_N 2048u
#define L2_GB 128
#define L2_CT (L2_GB + NLOC * 8)
#define NBMAX 484
#define NQ (NBMAX / 4)
#define QS 1040
#define LB_CNT (L2_CT + CT_N * 8)
#define LB_W (LB_CNT + 2048)
#define LB_OVF (LB_W + 16 + NQ * QS)
#define OVF_W (NQ - 112)
#define OVF_N ((16 - OVF_W) * 64)
static_assert(LB_OVF + OVF_N * 8 <= LDS_BYTES, "LDS map");
#define L2_ACC LB_CNT
#define L2_GA (L2_ACC + NLOC * 20)
#define L2_ACD (L2_GA + NLOC * 16)
static_assert(L2_GA < 65536 && L2_ACD + NLOC * 32 <= LDS_BYTES, "LDS map");
#define FIXBITS 30
#define SB() __builtin_amdgcn_sched_barrier(0)

__device__ u32x2 sbuf_load_b64(__amdgpu_buffer_rsrc_t, int, int, int, int) __asm("llvm.amdgcn.struct.ptr.buffer.load.v2i32");

__device__ __forceinline__ float bf2f(unsigned v) { return __uint_as_float(v << 16); }
__device__ __forceinline__ float bfhi(unsigned v) { return __uint_as_float(v & 0xffff0000u); }
__device__ __forceinline__ unsigned f2bf(float f) { return (unsigned)__builtin_bit_cast(u16, (__bf16)f); }
__device__ __forceinline__ float rbf16(float f) { return (float)(__bf16)f; }
__device__ __forceinline__ void lds_barrier() { asm volatile("s_waitcnt lgkmcnt(0)\n\ts_barrier" ::: "memory"); }
__device__ __forceinline__ float cosr(float f, float a) {
    const float x = rbf16(f * a), r = x * 0.15915494309189535f;
    const float lo = __builtin_fmaf(x, 6.4206382e-9f, __builtin_fmaf(x, 0.15915494309189535f, -r));
    return __builtin_amdgcn_cosf(__builtin_amdgcn_fractf(r) + lo);
}
__device__ __forceinline__ unsigned pk2(float lo, float hi) { bf16x2 v = {(__bf16)lo, (__bf16)hi}; return __builtin_bit_cast(unsigned, v); }
__device__ __forceinline__ u32x2 cos3(float f0, float f1, float f2, float a, bool& nan) {
    float c0 = cosr(f0, a), c1 = cosr(f1, a), c2 = cosr(f2, a), s = c0 + c1 + c2;
    nan = s != s;
    return (u32x2){pk2(c0, c1), pk2(c2, 0.f)};
}
__device__ __forceinline__ u64 mix64(u64 z) { z += 0x9e3779b97f4a7c15ull; z = (z ^ (z >> 30)) * 0xbf58476d1ce4e5b9ull; z = (z ^ (z >> 27)) * 0x94d049bb133111ebull; return z ^ (z >> 31); }
#define DPP_ADD(v, ctrl, rm) v += __builtin_amdgcn_update_dpp(0u, v, ctrl, rm, 0xf, false)
__device__ __forceinline__ unsigned row_scan(unsigned v) { DPP_ADD(v, 0x111, 0xf); DPP_ADD(v, 0x112, 0xf); DPP_ADD(v, 0x114, 0xf); DPP_ADD(v, 0x118, 0xf); return v; }
__device__ __forceinline__ unsigned wave_scan(unsigned v) { v = row_scan(v); DPP_ADD(v, 0x142, 0xa); DPP_ADD(v, 0x143, 0xc); return v; }
#define DPP_MAX(v, ctrl, rm) v = max(v, (unsigned)__builtin_amdgcn_update_dpp(0u, v, ctrl, rm, 0xf, false))
__device__ __forceinline__ unsigned wave_max(unsigned v) {
    DPP_MAX(v, 0x111, 0xf); DPP_MAX(v, 0x112, 0xf); DPP_MAX(v, 0x114, 0xf); DPP_MAX(v, 0x118, 0xf); DPP_MAX(v, 0x142, 0xa); DPP_MAX(v, 0x143, 0xc);
    return __builtin_amdgcn_readlane(v, 63);
}
__device__ __forceinline__ int ceil_exp(float x) { return __builtin_amdgcn_frexp_expf(x); }

struct Ctx { float f0, f1, f2; unsigned wd[8], alo; };

__device__ __forceinline__ void accum(const Ctx& x, unsigned la, unsigned ai, u32x2 g) {
    u32x2 c = *LP(u32x2, L2_CT + (ai & (CT_N - 1)) * 8);
    u32x4 ga = *LP(u32x4, 2 * la + L2_GA);
    u32x2 gb = *LP(u32x2, la + L2_GB);
    if (__builtin_expect(ai >= CT_N, 0)) {
        bool nan;
        c = cos3(x.f0, x.f1, x.f2, bf2f(ai + x.alo), nan);
    }
    unsigned c01 = c[0], c2l = c[1], c2h = c2l << 16;
    unsigned g0 = g[0], g1 = g[1], a0 = ga[0], a1 = ga[1], a2 = ga[2], a3 = ga[3], b0 = gb[0], b1 = gb[1];
    float x0, x1, x2, x3, u0, u1, u2, u3;
    asm("v_dot2_f32_bf16 %0, %9, %19, 0\n\t"
        "v_dot2_f32_bf16 %1, %9, %21, 0\n\t"
        "v_dot2_f32_bf16 %2, %9, %23, 0\n\t"
        "v_dot2_f32_bf16 %3, %9, %25, 0\n\t"
        "v_dot2_f32_bf16 %4, %11, %17, 0\n\t"
        "v_dot2_f32_bf16 %5, %12, %17, 0\n\t"
        "v_dot2_f32_bf16 %6, %11, %26, 0\n\t"
        "v_dot2_f32_bf16 %7, %12, %26, 0\n\t"
        "v_dot2_f32_bf16 %0, %8, %18, %0\n\t"
        "v_dot2_f32_bf16 %1, %8, %20, %1\n\t"
        "v_dot2_f32_bf16 %2, %8, %22, %2\n\t"
        "v_dot2_f32_bf16 %3, %8, %24, %3\n\t"
        "v_dot2_f32_bf16 %4, %10, %13, %4\n\t"
        "v_dot2_f32_bf16 %5, %10, %14, %5\n\t"
        "v_dot2_f32_bf16 %6, %10, %15, %6\n\t"
        "v_dot2_f32_bf16 %7, %10, %16, %7\n\t"
        "v_mul_f32 %0, %0, %4\n\t"
        "v_mul_f32 %1, %1, %5\n\t"
        "v_mul_f32 %2, %2, %6\n\t"
        "v_mul_f32 %3, %3, %7"
        : "=&v"(x0), "=&v"(x1), "=&v"(x2), "=&v"(x3), "=&v"(u0), "=&v"(u1), "=&v"(u2), "=&v"(u3)
        : "v"(g0), "v"(g1), "v"(c01), "v"(c2l), "v"(c2h), "v"(a0), "v"(a1), "v"(a2), "v"(a3), "v"(b0),
          "s"(x.wd[0]), "s"(x.wd[1]), "s"(x.wd[2]), "s"(x.wd[3]), "s"(x.wd[4]), "s"(x.wd[5]), "s"(x.wd[6]), "s"(x.wd[7]), "v"(b1));
    __hip_atomic_fetch_add(LP(double, la + L2_ACD), (double)x0, RLX_WG);
    __hip_atomic_fetch_add(LP(double, la + L2_ACD + NLOC * 8), (double)x1, RLX_WG);
    __hip_atomic_fetch_add(LP(double, la + L2_ACD + NLOC * 16), (double)x2, RLX_WG);
    __hip_atomic_fetch_add(LP(double, la + L2_ACD + NLOC * 24), (double)x3, RLX_WG);
}

typedef __attribute__((ext_vector_type(4))) short s16x4;
typedef __attribute__((ext_vector_type(4))) float f32x4;
#define NOREC 0xffc00000u
__device__ __forceinline__ void accum_fix(u32x2 wt, float mg, float mu, unsigned w, u32x2 g) {
    const unsigned la = (w >> 8) & 0x3ff8u;
    u32x2 c = *LP(u32x2, L2_CT + ((w << 3) & 0x3ff8u));
    u32x4 ga = *LP(u32x4, 2 * la + L2_GA);
    u32x2 gb = *LP(u32x2, la + L2_GB);
    f32x4 md = __builtin_amdgcn_mfma_f32_4x4x4bf16_1k(__builtin_bit_cast(s16x4, wt), __builtin_bit_cast(s16x4, g), (f32x4){0.f, 0.f, 0.f, 0.f}, 0, 0, 0);
    asm volatile("" : "+v"(md));
    unsigned c01 = c[0], c2l = c[1], a0 = ga[0], a1 = ga[1], a2 = ga[2], a3 = ga[3], b0 = gb[0], b1 = gb[1];
    float m0 = md[0], m1 = md[1], m2 = md[2], m3 = md[3], x0, x1, x2, x3, u0, u1, u2;
    asm("v_lshlrev_b32 %4, 16, %8\n\t"
        "v_dot2_f32_bf16 %0, %8, %13, 0\n\t"
        "v_dot2_f32_bf16 %2, %8, %14, 0\n\t"
        "v_dot2_f32_bf16 %1, %4, %13, 0\n\t"
        "v_dot2_f32_bf16 %3, %4, %14, 0\n\t"
        "v_dot2_f32_bf16 %0, %7, %9, %0\n\t"
        "v_dot2_f32_bf16 %2, %7, %11, %2\n\t"
        "v_dot2_f32_bf16 %1, %7, %10, %1\n\t"
        "v_dot2_f32_bf16 %3, %7, %12, %3\n\t"
        "v_mul_f32 %0, %0, %15\n\t"
        "v_mul_f32 %2, %2, %17\n\t"
        "v_mul_f32 %1, %1, %16\n\t"
        "v_mul_f32 %3, %3, %18\n\t"
        "v_max3_f32 %4, |%0|, |%1|, |%2|\n\t"
        "v_cvt_i32_f32 %0, %0\n\t"
        "v_cvt_i32_f32 %2, %2\n\t"
        "v_max_f32 %4, %4, |%3|\n\t"
        "v_cvt_i32_f32 %1, %1\n\t"
        "v_cvt_i32_f32 %3, %3\n\t"
        "v_ashrrev_i32 %5, 31, %0\n\t"
        "v_ashrrev_i32 %6, 31, %2\n\t"
        "v_fma_f32 %4, %4, %19, %20\n\t"
        "v_add_u32 %1, %1, %5\n\t"
        "v_add_u32 %3, %3, %6"
        : "=&v"(x0), "=&v"(x1), "=&v"(x2), "=&v"(x3), "=&v"(u0), "=&v"(u1), "=&v"(u2)
        : "v"(c01), "v"(c2l), "v"(a0), "v"(a1), "v"(a2), "v"(a3), "v"(b0), "v"(b1), "v"(m0), "v"(m1), "v"(m2), "v"(m3), "s"(mu), "v"(mg));
    if (w < NOREC) {
        __hip_atomic_fetch_add(LP(u64, la + L2_ACC), ((u64)__float_as_uint(x1) << 32) | __float_as_uint(x0), RLX_WG);
        __hip_atomic_fetch_add(LP(u64, la + L2_ACC + NLOC * 8), ((u64)__float_as_uint(x3) << 32) | __float_as_uint(x2), RLX_WG);
        __hip_atomic_fetch_add(LP(unsigned, (la >> 1) + L2_ACC + NLOC * 16), __float_as_uint(u0), RLX_WG);
    }
}
typedef __attribute__((ext_vector_type(2))) unsigned short u16x2;
__device__ __forceinline__ unsigned pkmax(unsigned a, unsigned b) { return __builtin_bit_cast(unsigned, __builtin_elementwise_max(__builtin_bit_cast(u16x2, a), __builtin_bit_cast(u16x2, b))); }
#define PL_A(h, r0, r1) \
    "v_lshrrev_b32 " r0 ", 5, " h "\n\tv_lshrrev_b32 " r1 ", 21, " h "\n\tv_and_b32 " r0 ", 0x7fc, " r0 "\n\tv_and_b32 " r1 ", 0x7fc, " r1 "\n\t"
#define PL_A2(m, r0, r1) \
    "s_mov_b64 exec, " m "\n\tds_add_rtn_u32 " r0 ", " r0 ", %[inc] offset:%[oc]\n\tds_add_rtn_u32 " r1 ", " r1 ", %[inc] offset:%[oc]\n\t"
#define PL_B(m, n, wa, wb, r0, r1) \
    "s_mov_b64 exec, " m "\n\ts_waitcnt lgkmcnt(" n ")\n\t" \
    "v_min_u32 %[t0], %[lim], " r0 "\n\tv_min_u32 %[t1], %[lim], " r1 "\n\tv_max3_u32 %[mx], %[mx], " r0 ", " r1 "\n\t" \
    "v_and_b32 %[t0], 0x3ffff, %[t0]\n\tv_and_b32 %[t1], 0x3ffff, %[t1]\n\t" \
    "ds_write_b32 %[t0], " wa " offset:%[ow]\n\tds_write_b32 %[t1], " wb " offset:%[ow]\n\t"
#define FULL (64u << 18)
__device__ __forceinline__ void place4(u32x3 p0, u32x3 p1, u32x3 p2, u32x3 p3) {
    unsigned r[8], t0, t1, mx = 0; u64 m0, m1, m2, m3;
    asm volatile(
        "v_cmp_ne_u32 %[m0], 0, %[h0]\n\tv_cmp_ne_u32 %[m1], 0, %[h1]\n\tv_cmp_ne_u32 %[m2], 0, %[h2]\n\tv_cmp_ne_u32 %[m3], 0, %[h3]\n\t"
        PL_A("%[h0]", "%[r0]", "%[r1]") PL_A("%[h1]", "%[r2]", "%[r3]") PL_A("%[h2]", "%[r4]", "%[r5]") PL_A("%[h3]", "%[r6]", "%[r7]")
        PL_A2("%[m0]", "%[r0]", "%[r1]") PL_A2("%[m1]", "%[r2]", "%[r3]") PL_A2("%[m2]", "%[r4]", "%[r5]") PL_A2("%[m3]", "%[r6]", "%[r7]")
        PL_B("%[m0]", "6", "%[a0]", "%[b0]", "%[r0]", "%[r1]")
        PL_B("%[m1]", "6", "%[a1]", "%[b1]", "%[r2]", "%[r3]")
        PL_B("%[m2]", "6", "%[a2]", "%[b2]", "%[r4]", "%[r5]")
        PL_B("%[m3]", "6", "%[a3]", "%[b3]", "%[r6]", "%[r7]")
        "s_mov_b64 exec, -1"
        : [r0] "=&v"(r[0]), [r1] "=&v"(r[1]), [r2] "=&v"(r[2]), [r3] "=&v"(r[3]), [r4] "=&v"(r[4]), [r5] "=&v"(r[5]), [r6] "=&v"(r[6]), [r7] "=&v"(r[7]),
          [t0] "=&v"(t0), [t1] "=&v"(t1), [mx] "+&v"(mx), [m0] "=&s"(m0), [m1] "=&s"(m1), [m2] "=&s"(m2), [m3] "=&s"(m3)
        : [a0] "v"(p0[0]), [h0] "v"(p0[1]), [b0] "v"(p0[2]), [a1] "v"(p1[0]), [h1] "v"(p1[1]), [b1] "v"(p1[2]),
          [a2] "v"(p2[0]), [h2] "v"(p2[1]), [b2] "v"(p2[2]), [a3] "v"(p3[0]), [h3] "v"(p3[1]), [b3] "v"(p3[2]),
          [inc] "v"(0x40010u), [lim] "s"(FULL), [oc] "n"(LB_CNT), [ow] "n"(LB_W)
        : "memory");
    if (__any(mx >= FULL)) {
        const unsigned w[8] = {p0[0], p0[2], p1[0], p1[2], p2[0], p2[2], p3[0], p3[2]};
        const unsigned h[4] = {p0[1], p1[1], p2[1], p3[1]};
        u64 mk[8]; unsigned tot = 0;
#pragma unroll
        for (int i = 0; i < 8; ++i) { mk[i] = __ballot(r[i] >= FULL); tot += __popcll(mk[i]); }
        unsigned pos = 0;
        if ((threadIdx.x & 63) == 0) pos = __hip_atomic_fetch_add(LP(unsigned, C_OVF), tot, RLX_WG);
        pos = __builtin_amdgcn_readfirstlane(pos);
        if (pos + tot <= OVF_N) {
#pragma unroll
            for (int i = 0; i < 8; ++i) {
                if (mk[i]) {
                    unsigned p = __builtin_amdgcn_mbcnt_hi((unsigned)(mk[i] >> 32), __builtin_amdgcn_mbcnt_lo((unsigned)mk[i], pos));
                    if (r[i] >= FULL) *LP(u32x2, LB_OVF + p * 8) = (u32x2){w[i], (i & 1) ? h[i >> 1] >> 16 : h[i >> 1]};
                    pos += __popcll(mk[i]);
                }
            }
        }
    }
}
__device__ __forceinline__ bool rec_ok(unsigned h) { return h & 16u; }
__device__ __forceinline__ unsigned rec_row(unsigned w, unsigned h, unsigned bw) { return rec_ok(h) ? __umul24((h >> 7) & 0x1ffu, bw) + (w >> 22) : 0x7fffffffu; }
__device__ __forceinline__ unsigned rec_la(unsigned w) { return (w >> 8) & 0x3ff8u; }
__device__ __forceinline__ unsigned rec_ai(unsigned w, unsigned h) { return (w & 0x7ffu) | ((h & 15u) << 11); }

__device__ __forceinline__ u32x3 fetch(unsigned& todo, unsigned& sg0, unsigned sg1, const unsigned* recs, unsigned po) {
    unsigned off = 0, end = 0;
    if (todo) {
        int k = __builtin_ctz(todo);
        off = __builtin_amdgcn_readlane(sg0, k); end = __builtin_amdgcn_readlane(sg1, k);
        if (__builtin_expect(end - off > 768, 0)) sg0 = ((int)(threadIdx.x & 63) == k) ? off + 768 : sg0;
        else todo &= todo - 1;
    }
    __amdgpu_buffer_rsrc_t rs = __builtin_amdgcn_make_buffer_rsrc((void*)recs, 0, end, 0x00020000);
    return __builtin_amdgcn_raw_buffer_load_b96(rs, off + po, 0, 16);
}

__global__ void __launch_bounds__(NT, 4) k_fused(const int* __restrict__ idx_in, const int* __restrict__ idx_out, const u16* __restrict__ angle,
                                              const u16* __restrict__ m, const u16* __restrict__ dist, const u16* __restrict__ centers, const u16* __restrict__ freq,
                                              const u16* __restrict__ w_rbf, const u16* __restrict__ w_sbf, const u16* __restrict__ w_down, const u16* __restrict__ w_up,
                                              u16* __restrict__ out, unsigned* recs, u64* tab, u64* rdy, u64* tagword,
                                              int T, int E, int chunk, int rec_stride, unsigned bmul, int bw  ) {
    const int tid = threadIdx.x, lane = tid & 63, wave = __builtin_amdgcn_readfirstlane(tid >> 6);
    const int b = blockIdx.x;
    const int bk = (b & 7) * 32 + (b >> 3);
    const int t0 = b * chunk;
    int n = T - t0; n = n < 0 ? 0 : (n > chunk ? chunk : n);
    const int nq4 = n >> 2;
    unsigned pf = 0;
    if (wave >= 10) {
        u64 pc; unsigned len;
        asm volatile("s_getpc_b64 %0\n.Lgem_here:\n\ts_mov_b32 %1, .Lgem_end-.Lgem_here" : "=s"(pc), "=s"(len));
        if ((unsigned)(tid - 640) * 128 < len) pf = *(const GAS unsigned*)(pc + (size_t)(tid - 640) * 128);
    }
    SB();
    if (tid < 256) *LP(unsigned, L1_H + tid * 4) = 0;
    if (tid < 8) *LP(unsigned, L_CTL + tid * 4) = 0;
    __amdgpu_buffer_rsrc_t ro = __builtin_amdgcn_make_buffer_rsrc((void*)(idx_out + t0), 0, n * 4, 0x00020000);
    __amdgpu_buffer_rsrc_t ri = __builtin_amdgcn_make_buffer_rsrc((void*)(idx_in + t0), 0, n * 4, 0x00020000);
    __amdgpu_buffer_rsrc_t ra = __builtin_amdgcn_make_buffer_rsrc((void*)(angle + t0), 0, n * 2, 0x00020000);
    u32x4 vo[QUADS], vi[QUADS]; u32x2 va[QUADS];
    SB();
#pragma unroll
    for (int q = 0; q < QUADS; ++q) { if (q == 1) lds_barrier(); vo[q] = __builtin_amdgcn_raw_buffer_load_b128(ro, (q * NT + tid) * 16, 0, 0); SB(); }
    u64 tag0 = __hip_atomic_load((GAS u64*)tagword, RLX_AGENT);
    unsigned dd[2], cv; u32x4 wa;
#pragma unroll
    for (int j = 0; j < 2; ++j) {
        int l = tid + j * 1024;
        int e = ((l >> 4) << 12) | (bk << 4) | (l & 15);
        dd[j] = dist[e < E ? e : 0];
    }
    cv = *(lane < 8 ? centers + lane : freq + (lane < 11 ? lane - 8 : 0));
    {
        int o = (lane & 3) + 4 * ((lane & 31) >> 3);
        const u16* p = o < 12 ? w_sbf + o : w_rbf + (o - 12);
        int st = o < 12 ? 12 : 4;
#pragma unroll
        for (int i = 0; i < 4; ++i) wa[i] = (unsigned)p[(2 * i) * st] | ((unsigned)p[(2 * i + 1) * st] << 16);
        if (((lane >> 2) & 1) != (lane >> 5)) wa = (u32x4){0, 0, 0, 0};
    }
    SB();
    u32x8 wdn = *(const u32x8*)w_down, wup = *(const u32x8*)w_up;
    SB();
    SB();
    u32x4 rk[QUADS];
#pragma unroll
    for (int q = 0; q < QUADS; ++q) {
        u32x4 r = {0, 0, 0, 0};
        if (q * NT + tid < nq4) {
#pragma unroll
            for (int k = 0; k < 4; ++k) {
                const unsigned o = vo[q][k], ad = (o >> 2) & 0x3fcu;
                r[k] = __hip_atomic_fetch_add(LP(unsigned, L1_H + ad), 1u, RLX_WG);
                unsigned u;
                asm("v_bfi_b32 %0, %1, %2, %3" : "=v"(u) : "s"(0x3fcu), "v"(ad), "v"(__umul24(o & 0x7f00fu, 0x808u)));
                vo[q][k] = u;
            }
#pragma unroll
            for (int k = 0; k < 4; ++k) {
                const unsigned t = __umul24(r[k], 3u) + 1u;
                r[k] = __umul24(t & 1u, 0x60000u) + (t >> 1);
            }
        }
        rk[q] = r;
        asm volatile("" : "+v"(rk[q]), "+v"(vo[q]));
        SB();
        if (q >= QUADS - 1 - AHEAD && q < QUADS - 1) {
            int p = q - (QUADS - 1 - AHEAD);
            vi[p] = __builtin_amdgcn_raw_buffer_load_b128(ri, (p * NT + tid) * 16, 0, 0);
            va[p] = __builtin_amdgcn_raw_buffer_load_b64(ra, (p * NT + tid) * 8, 0, 0);
        }
        SB();
    }
    asm volatile("" : "+s"(wdn), "+s"(wup));
    Ctx x;
    x.f0 = bf2f(__builtin_amdgcn_readlane(cv, 8)); x.f1 = bf2f(__builtin_amdgcn_readlane(cv, 9)); x.f2 = bf2f(__builtin_amdgcn_readlane(cv, 10));
    const float fmx = fmaxf(fmaxf(fabsf(x.f0), fabsf(x.f1)), fabsf(x.f2)) + (x.f0 + x.f1 + x.f2) * 0.f;
    const int elo = min(122 - ceil_exp(fmx), 238);
    const bool fok = fmx == fmx && elo >= 1;
    x.alo = __builtin_amdgcn_readfirstlane(fok ? (unsigned)elo << 7 : 0u);
    const u64 tagA = mix64(((u64)__builtin_amdgcn_readfirstlane((unsigned)(tag0 >> 32)) << 32) | (unsigned)__builtin_amdgcn_readfirstlane((unsigned)tag0));
    const u64 tagB = mix64(tagA);
    lds_barrier();
    unsigned npair;
    {
        u32x4 h = *LP(u32x4, L1_H + lane * 16);
        u32x4 e = (h + 1u) & ~1u;
        unsigned s = e[0] + e[1] + e[2] + e[3];
        unsigned inc = wave_scan(s);
        npair = __builtin_amdgcn_readlane(inc, 63) >> 1;
        u32x4 p; p[0] = inc - s; p[1] = p[0] + e[0]; p[2] = p[1] + e[1]; p[3] = p[2] + e[2];
        *LP(u32x4, L1_BS + lane * 16) = p * 6u;
        if (wave == 15) {
#pragma unroll
            for (int i = 0; i < 4; ++i) if (h[i] & 1u) {
                unsigned pd = (p[i] + e[i]) * 6 - 4 + L1_BUF;
                *LP(unsigned, pd) = NOREC; *LP(u16, pd - 2) = (u16)(b << 7);
            }
        }
        asm volatile("s_waitcnt lgkmcnt(0)" ::: "memory");
    }
    u32x2 mv[2];
    unsigned amax = 0;
    u32x4 bsq[QUADS];
    asm volatile("" : "+v"(vo[0]));
#pragma unroll
    for (int k = 0; k < 4; ++k) bsq[0][k] = *LP(unsigned, L1_BS + (vo[0][k] & 0x3fcu));
#pragma unroll
    for (int q = 0; q < QUADS; ++q) {
        if (q + AHEAD < QUADS) {
            int p = q + AHEAD;
            vi[p] = __builtin_amdgcn_raw_buffer_load_b128(ri, (p * NT + tid) * 16, 0, 0);
            va[p] = __builtin_amdgcn_raw_buffer_load_b64(ra, (p * NT + tid) * 8, 0, 0);
        }
        if (q == QUADS - 1) {
#pragma unroll
            for (int j = 0; j < 2; ++j) {
                int l = tid + j * 1024;
                int e = ((l >> 4) << 12) | (bk << 4) | (l & 15);
                mv[j] = *(const u32x2*)(m + (size_t)(e < E ? e : 0) * 4);
            }
        }
        if (q + 1 < QUADS) {
            asm volatile("" : "+v"(vo[q + 1]));
#pragma unroll
            for (int k = 0; k < 4; ++k) bsq[q + 1][k] = *LP(unsigned, L1_BS + (vo[q + 1][k] & 0x3fcu));
        }
        SB();
        if (q * NT + tid < nq4) {
            unsigned A[2];
            const u32x4 bs = bsq[q];
#pragma unroll
            for (int k = 0; k < 2; ++k) { A[k] = pkmax(va[q][k] & 0x7fff7fffu, x.alo * 0x10001u) + (0x8000u - x.alo) * 0x10001u; amax |= A[k]; }
#pragma unroll
            for (int k = 0; k < 4; ++k) {
                unsigned o = vo[q][k], in = vi[q][k], bb = bs[k], rr = rk[q][k], bin, w, a, t, wd;
                asm("v_mad_u32_u16 %0, %1, 4, %2" : "=v"(w) : "v"(rr), "v"(bb));
                asm("v_mad_u32_u16 %0, %1, 1, %2 op_sel:[1,0,0,0]" : "=v"(a) : "v"(rr), "v"(w));
                asm("v_bfi_b32 %0, %1, %2, %3" : "=v"(t) : "s"(0x7ffu), "v"((k & 1) ? A[k >> 1] >> 16 : A[k >> 1]), "v"(o));
                asm("v_mul_hi_u32_u24 %0, %1, %2" : "=v"(bin) : "s"(bmul), "v"(in));
                asm("v_mad_i32_i24 %0, %1, %2, %3" : "=v"(in) : "v"(bin), "s"(-bw), "v"(in));
                asm("v_bfi_b32 %0, %1, %2, %3" : "=v"(wd) : "s"(0x3fffffu), "v"(t), "v"(in << 22));
                *LP(unsigned, w + L1_BUF) = wd;
                *LP(u16, a + (L1_BUF - 2)) = (u16)((bin << 7) | ((A[k >> 1] >> ((k & 1) ? 27 : 11)) & 31u));
            }
        }
        SB();
    }
    asm volatile("" : "+v"(va[QUADS - 1]), "+v"(pf), "+v"(dd[0]), "+v"(dd[1]), "+v"(wa), "+v"(cv));
    lds_barrier();
    const unsigned tword = (__umul24(*LP(unsigned, L1_BS + (tid & 255) * 4) >> 2, 21846u) >> 16) | (((*LP(unsigned, L1_H + (tid & 255) * 4) + 1) >> 1) << 14);
    u32x4 st[NST];
    __amdgpu_buffer_rsrc_t rd = __builtin_amdgcn_make_buffer_rsrc((void*)(recs + (size_t)b * rec_stride), 0, __builtin_amdgcn_readfirstlane(((npair * 3 + 3) / 4) * 16), 0x00020000);
#pragma unroll
    for (int k = 0; k < NST; ++k) st[k] = *LP(u32x4, L1_BUF + (k * NT + tid) * 16);
    lds_barrier();
    {
        __amdgpu_buffer_rsrc_t rw = __builtin_amdgcn_make_buffer_rsrc((void*)(tab + (size_t)b * NWG), 0, NWG * 8, 0x00020000);
        const u64 v = (tagA << 28) | tword;
        __builtin_amdgcn_raw_buffer_store_b64((u32x2){(unsigned)v, (unsigned)(v >> 32)}, rw, tid * 8, 0, 16);
    }
#pragma unroll
    for (int k = 0; k < NS1; ++k) __builtin_amdgcn_raw_buffer_store_b128(st[k], rd, (k * NT + tid) * 16, 0, 16);
    SB();
#pragma unroll
    for (int h = 0; h < 2; ++h) {
        unsigned r0a = wdn[4 * h], r0b = wdn[4 * h + 1], r1a = wdn[4 * h + 2], r1b = wdn[4 * h + 3];
        x.wd[0 + h] = (r0a & 0xffffu) | (r1a << 16);
        x.wd[2 + h] = (r0a >> 16) | (r1a & 0xffff0000u);
        x.wd[4 + h] = (r0b & 0xffffu) | (r1b << 16);
        x.wd[6 + h] = (r0b >> 16) | (r1b & 0xffff0000u);
    }
    u32x2 ctv[CT_N / NT]; unsigned gp[2][6]; int es[2]; float gate[2][4];
    bool odd = !fok;
#pragma unroll
    for (int k = 0; k < CT_N / NT; ++k) { bool nan; ctv[k] = cos3(x.f0, x.f1, x.f2, bf2f(x.alo + k * NT + tid), nan); odd |= nan; asm volatile("" : "+v"(ctv[k])); }
#pragma unroll
    for (int j = 0; j < 2; ++j) {
#pragma unroll
        for (int d = 0; d < 4; ++d) gate[j][d] = 0.f;
#pragma unroll
        for (int i = 0; i < 6; ++i) gp[j][i] = 0;
        es[j] = 0;
        if (j * 1024 + wave * 64 < NLOC) {
            float ev[8], chk = 0.f;
            const float dv = bf2f(dd[j]);
#pragma unroll
            for (int i = 0; i < 4; ++i) {
                unsigned t = pk2(dv - bf2f(__builtin_amdgcn_readlane(cv, 2 * i)), dv - bf2f(__builtin_amdgcn_readlane(cv, 2 * i + 1)));
                float d0 = bf2f(t), d1 = bfhi(t);
                t = pk2(d0 * d0, d1 * d1);
                t = pk2(-10.f * bf2f(t), -10.f * bfhi(t));
                ev[2 * i] = __expf(bf2f(t)); ev[2 * i + 1] = __expf(bfhi(t));
                chk += ev[2 * i] + ev[2 * i + 1];
            }
            const bool bad = chk != chk;
            const int e = max(ceil_exp(chk), -100) + 1;
            const float sc = __uint_as_float((unsigned)(127 - e) << 23);
            const unsigned keep = bad ? 0u : ~0u;
            u32x4 rb;
#pragma unroll
            for (int i = 0; i < 4; ++i) rb[i] = pk2(ev[2 * i] * sc, ev[2 * i + 1] * sc) & keep;
            f32x16 z = {0.f, 0.f, 0.f, 0.f, 0.f, 0.f, 0.f, 0.f, 0.f, 0.f, 0.f, 0.f, 0.f, 0.f, 0.f, 0.f};
            f32x16 g = __builtin_amdgcn_mfma_f32_32x32x16_bf16(__builtin_bit_cast(bf16x8, wa), __builtin_bit_cast(bf16x8, rb), z, 0, 0, 0);
            const float us = bad ? chk : __uint_as_float((unsigned)(127 + e) << 23);
            es[j] = e;
#pragma unroll
            for (int d = 0; d < 4; ++d) { gate[j][d] = g[12 + d] * us; gp[j][d] = pk2(g[d], g[4 + d]); }
            gp[j][4] = pk2(g[8], g[9]); gp[j][5] = pk2(g[10], g[11]);
        }
    }
    if (wave == 15) {
        unsigned wm = pkmax(pkmax(wa[0] & 0x7f807f80u, wa[1] & 0x7f807f80u), pkmax(wa[2] & 0x7f807f80u, wa[3] & 0x7f807f80u));
        wm = max(wm & 0xffffu, wm >> 16) >> 7;
        odd |= wm == 255;
        u32x4 we;
#pragma unroll
        for (int d = 0; d < 4; ++d) {
            unsigned es3 = max(max(__builtin_amdgcn_readlane(wm, d), __builtin_amdgcn_readlane(wm, 8 + d)), __builtin_amdgcn_readlane(wm, 16 + d));
            odd |= es3 > 157 || (es3 && es3 < 97); es3 = max(es3, 97u);
            unsigned t = pkmax(x.wd[2 * d] & 0x7f807f80u, x.wd[2 * d + 1] & 0x7f807f80u);
            t = max(t & 0xffffu, t >> 16) >> 7;
            odd |= t == 255;
            we[d] = (t - 126 + 2) + (es3 - 126 + 1) - 126;
        }
        *LP(u32x4, C_WE) = we;
        *LP(u32x4, C_WD) = (u32x4){x.wd[0], x.wd[1], x.wd[2], x.wd[3]}; *LP(u32x4, C_WD + 16) = (u32x4){x.wd[4], x.wd[5], x.wd[6], x.wd[7]};
    }
    if (odd) *LP(unsigned, C_FAIL) = 1u;
#pragma unroll
    for (int j = 0; j < 2; ++j) {
#pragma unroll
        for (int i = 0; i < 6; ++i) asm volatile("" : "+v"(gp[j][i]));
#pragma unroll
        for (int d = 0; d < 4; ++d) asm volatile("" : "+v"(gate[j][d]));
        asm volatile("" : "+v"(es[j]));
    }
    SB();
#pragma unroll
    for (int k = NS1; k < NST; ++k) __builtin_amdgcn_raw_buffer_store_b128(st[k], rd, (k * NT + tid) * 16, 0, 16);
    SB();
#pragma unroll
    for (int k = 0; k < 8; ++k) if (k * NT + tid < NQ * QS / 16) *LP(u32x4, LB_W + 16 + (k * NT + tid) * 16) = (u32x4){NOREC, NOREC, NOREC, NOREC};
    if (tid < NBMAX) *LP(unsigned, LB_CNT + tid * 4) = (tid >> 2) * QS + (tid & 3) * 4 + 16;
    if (tid < OVF_N) *LP(u32x2, LB_OVF + tid * 8) = (u32x2){NOREC, 0};
#pragma unroll
    for (int k = 0; k < CT_N / NT; ++k) *LP(u32x2, L2_CT + (k * NT + tid) * 8) = ctv[k];
#pragma unroll
    for (int j = 0; j < 2; ++j) if (tid + j * 1024 < NLOC) *LP(u32x2, L2_GB + (tid + j * 1024) * 8) = (u32x2){gp[j][4], gp[j][5]};
    SB();
    {
        unsigned mx = 0, mn = 0;
#pragma unroll
        for (int j = 0; j < 2; ++j) {
            unsigned a = mv[j][0], c = mv[j][1];
            unsigned r = max(max(a & 0x7fffu, (a >> 16) & 0x7fffu), max(c & 0x7fffu, (c >> 16) & 0x7fffu));
            mx = max(mx, r >> 7);
            mn = max(mn, r ? 255u - (r >> 7) : 0u);
        }
        if (amax & 0x78007800u) mx = 255;
        mx = wave_max(mx); mn = wave_max(mn);
        if (lane == 0) { __hip_atomic_fetch_max(LP(unsigned, C_MAXE), mx, RLX_WG); __hip_atomic_fetch_max(LP(unsigned, C_MINE), mn, RLX_WG); }
    }
    lds_barrier();
    __amdgpu_buffer_rsrc_t rt = __builtin_amdgcn_make_buffer_rsrc((void*)(tab + (size_t)wave * 16 * NWG + bk), 0, 15 * NWG * 8 + 8, 0x00020000);
    const unsigned lt = lane * NWG * 8;
    __amdgpu_buffer_rsrc_t rr = __builtin_amdgcn_make_buffer_rsrc((void*)(rdy + (b & 7) * NWG + wave * 16), 0, 128, 0x00020000);
    u32x2 t = __builtin_amdgcn_raw_buffer_load_b64(rr, lane * 8, 0, 16);
    u32x2 tw = __builtin_amdgcn_raw_buffer_load_b64(rt, lt, 0, 16);
    {
        __builtin_amdgcn_s_setprio(3);
        asm volatile("s_waitcnt vmcnt(2)" ::: "memory");
        unsigned arr = 0;
        if (lane == 0) arr = __hip_atomic_fetch_add(LP(unsigned, C_ARR), 1u, RLX_WG);
        if (__builtin_amdgcn_readfirstlane(arr) == 15) {
            const u64 v = (tagB << 16) | (*LP(unsigned, C_MINE) << 8) | *LP(unsigned, C_MAXE);
            if (lane < 8) __hip_atomic_store((GAS u64*)rdy + lane * NWG + b, v, RLX_AGENT);
        }
    }
    unsigned sg0 = 0, sg1 = 0;
    int qk[8];
#pragma unroll
    for (int k = 0; k < 8; ++k) {
        int q = wave + 16 * k + (((b >> 3) & 31) * 15) % NQ;
        q = q >= NQ ? q - NQ : q;
        qk[k] = q >= NQ ? q - NQ : q;
        asm volatile("" : "+s"(qk[k]));
    }
    {
        const unsigned ta0 = (unsigned)tagA & 0xfu, ta1 = (unsigned)(tagA >> 4), tb0 = (unsigned)tagB & 0xffffu, tb1 = (unsigned)(tagB >> 16);
        unsigned seen = 0, spins = 0, cur = 0, ex = 0;
        __builtin_amdgcn_s_setprio(2);
        unsigned lo = lt;
        auto look = [&]() {
            asm volatile("" : "+v"(t), "+v"(tw));
            if ((((tw[0] >> 28) ^ ta0) | (tw[1] ^ ta1)) == 0 && lane < 16 && lo == lt) {
                sg0 = min(((unsigned)(wave * 16 + lane) * rec_stride + (tw[0] & 0x3fffu) * 3) * 4, (unsigned)(NWG * rec_stride * 4));
                sg1 = min(sg0 + ((tw[0] >> 14) & 0x3fffu) * 12, (unsigned)(NWG * rec_stride * 4));
                cur = sg0;
                lo = 0x7ffffff0u;
            }
            const bool ok = (((t[0] >> 16) ^ tb0) | (t[1] ^ tb1)) == 0 && lo == 0x7ffffff0u;
            seen |= (unsigned)__ballot(ok) & 0xffffu;
            if (ok) ex = t[0];
        };
        auto await = [&](int need) {
#pragma unroll 1
            while (__builtin_popcount(seen) < need) {
                if (++spins >= (1u << 16)) { seen = 0xffffu; *LP(unsigned, C_FAIL) = 1u; }
                __builtin_amdgcn_s_sleep(16);
                t = __builtin_amdgcn_raw_buffer_load_b64(rr, lane * 8, 0, 16);
                tw = __builtin_amdgcn_raw_buffer_load_b64(rt, lo, 0, 16);
                look();
            }
        };
        auto poll = [&]() {
            t = __builtin_amdgcn_raw_buffer_load_b64(rr, seen != 0xffffu ? lane * 8 : 0x7ffffff0u, 0, 16);
            tw = __builtin_amdgcn_raw_buffer_load_b64(rt, lo, 0, 16);
            SB();
        };
        auto issue = [&](u32x3 (&rc)[4]) {
            unsigned todo = (unsigned)__ballot(cur < sg1) & seen;
            const unsigned all = todo;
#pragma unroll
            for (int i = 0; i < 4; ++i) {
                const int k = __builtin_ctz(todo | 0x10000u);
                todo &= todo - 1;
                __amdgpu_buffer_rsrc_t rs = __builtin_amdgcn_make_buffer_rsrc((void*)recs, 0, __builtin_amdgcn_readlane(sg1, k), 0x00020000);
                rc[i] = __builtin_amdgcn_raw_buffer_load_b96(rs, lane * 12, __builtin_amdgcn_readlane(cur, k), 16);
                SB();
            }
            if (((all & ~todo) >> (lane & 31)) & 1u) cur = min(cur + 768, sg1);
        };
        u32x3 ra[4], rb[4], rc[4];
        look();
        await(8);
        issue(ra);
        poll();
        issue(rb);
        __builtin_amdgcn_s_setprio(1);
        place4(ra[0], ra[1], ra[2], ra[3]);
        SB();
        look();
        await(12);
        poll();
        issue(ra);
        __builtin_amdgcn_s_setprio(0);
        place4(rb[0], rb[1], rb[2], rb[3]);
        SB();
        look();
        await(16);
        issue(rb);
        const bool more = __any(cur < sg1);
        if (more) issue(rc);
        ex = (ex & 255u) | ((ex & 0xff00u) << 8);
        ex = pkmax(ex, __builtin_amdgcn_update_dpp(0u, ex, 0x111, 0xf, 0xf, false)); ex = pkmax(ex, __builtin_amdgcn_update_dpp(0u, ex, 0x112, 0xf, 0xf, false));
        ex = pkmax(ex, __builtin_amdgcn_update_dpp(0u, ex, 0x114, 0xf, 0xf, false)); ex = pkmax(ex, __builtin_amdgcn_update_dpp(0u, ex, 0x118, 0xf, 0xf, false));
        if (lane == 15) { __hip_atomic_fetch_max(LP(unsigned, C_GMAX), ex & 0xffffu, RLX_WG); __hip_atomic_fetch_max(LP(unsigned, C_GMIN), ex >> 16, RLX_WG); }
        SB();
        place4(ra[0], ra[1], ra[2], ra[3]);
        SB();
        place4(rb[0], rb[1], rb[2], rb[3]);
        SB();
        if (more) {
            place4(rc[0], rc[1], rc[2], rc[3]);
#pragma unroll 1
            while (__any(cur < sg1) && __builtin_amdgcn_readfirstlane(__hip_atomic_load(LP(unsigned, C_OVF), RLX_WG)) <= OVF_N) {
                issue(rc);
                place4(rc[0], rc[1], rc[2], rc[3]);
            }
        }
    }
    lds_barrier();
    const u32x4 ctl = *LP(u32x4, L_CTL);
    const unsigned wel = *LP(unsigned, C_WE + (lane & 3) * 4);
    const u32x2 wdl = *LP(u32x2, C_WD + (lane & 3) * 8);
    u32x4 W[8]; u32x2 ov;
#pragma unroll
    for (int k = 0; k < 8; ++k) W[k] = *LP(u32x4, LB_W + 16 + qk[k] * QS + lane * 16);
    ov = *LP(u32x2, LB_OVF + (lane * (16 - OVF_W) + (wave >= OVF_W ? wave - OVF_W : 0)) * 8);
    bool fast; int em[4];
    u32x2 wt;
    {
        const int gmx = __builtin_amdgcn_readfirstlane(ctl[2]), gmn = 255 - (int)__builtin_amdgcn_readfirstlane(ctl[3]);
        fast = !__builtin_amdgcn_readfirstlane(ctl[1]) && __builtin_amdgcn_readfirstlane(ctl[0]) <= OVF_N && gmx >= 67 && gmx <= 187 && gmx - gmn <= 14;
        const int k = FIXBITS - gmx - (int)wel;
        wt = (u32x2){pk2(ldexpf(bf2f(wdl[0]), k), ldexpf(bfhi(wdl[0]), k)), pk2(ldexpf(bf2f(wdl[1]), k), ldexpf(bfhi(wdl[1]), k))};
#pragma unroll
        for (int d = 0; d < 4; ++d) em[d] = -__builtin_amdgcn_readlane(k, d);
    }
    auto set_up = [&]() {
        lds_barrier();
#pragma unroll
        for (int j = 0; j < 2; ++j) if (tid + j * 1024 < NLOC) *LP(u32x4, L2_GA + (tid + j * 1024) * 16) = (u32x4){gp[j][0], gp[j][1], gp[j][2], gp[j][3]};
#pragma unroll
        for (int k = 0; k < 2; ++k) if (k * NT + tid < NLOC * 5 / 4) *LP(u32x4, L2_ACC + (k * NT + tid) * 16) = (u32x4){0, 0, 0, 0};
        lds_barrier();
    };
    auto finalize = [&](bool fix) {
        float wu[16];
#pragma unroll
        for (int i = 0; i < 16; ++i) wu[i] = (i & 1) ? bfhi(wup[i >> 1]) : bf2f(wup[i >> 1]);
#pragma unroll
        for (int j = 0; j < 2; ++j) {
            int l = tid + j * 1024;
            int e = ((l >> 4) << 12) | (bk << 4) | (l & 15);
            if (e < E) {
                float a[4];
                if (fix) {
#pragma unroll
                    for (int h = 0; h < 2; ++h) {
                        u32x2 raw = *LP(u32x2, L2_ACC + (h * NLOC + l) * 8);
                        int lo = (int)raw[0], hi = (int)raw[1] - (lo >> 31);
                        a[2 * h] = ldexpf((float)lo, es[j] + em[2 * h]); a[2 * h + 1] = ldexpf((float)hi, es[j] + em[2 * h + 1]);
                    }
                    if ((*LP(unsigned, L2_ACC + NLOC * 16 + l * 4) & 0xffffffu) >= 512u) *LP(unsigned, C_FAIL) = 1u;
                } else {
#pragma unroll
                    for (int d = 0; d < 4; ++d) a[d] = (float)ldexp(*LP(double, L2_ACD + (d * NLOC + l) * 8), es[j]);
                }
                float mm[4] = {bf2f(mv[j][0]), bfhi(mv[j][0]), bf2f(mv[j][1]), bfhi(mv[j][1])};
                unsigned o[4];
#pragma unroll
                for (int d = 0; d < 4; ++d) {
                    float u = a[0] * wu[d] + a[1] * wu[4 + d] + a[2] * wu[8 + d] + a[3] * wu[12 + d];
                    o[d] = f2bf(mm[d] + u * gate[j][d]);
                }
                __builtin_nontemporal_store((u32x2){o[0] | (o[1] << 16), o[2] | (o[3] << 16)}, (u32x2*)(out + (size_t)e * 4));
            }
        }
    };
    if (b == 0 && tid == 0) __hip_atomic_store((GAS u64*)tagword, tagA, RLX_AGENT);
    __amdgpu_buffer_rsrc_t rm = __builtin_amdgcn_make_buffer_rsrc((void*)m, 8, E, 0x00020000);
    if (fast) {
        const u16* m2 = m; int bw2 = bw;
        float mg = 8388609.f, mu = 0x1p-22f;
        asm volatile("" : "+s"(m2), "+s"(bw2), "+v"(mg), "+s"(mu));
        __amdgpu_buffer_rsrc_t rb = __builtin_amdgcn_make_buffer_rsrc((void*)m2, 8, bw2, 0x00020000);
        u32x2 g[GD];
        auto gather = [&](int s) { g[s % GD] = sbuf_load_b64(rb, W[s >> 2][s & 3] >> 22, 0, ((qk[s >> 2] << 2) + (s & 3)) * bw * 8, 0); SB(); };
        auto add = [&](int c) { accum_fix(wt, mg, mu, W[c >> 2][c & 3], g[c % GD]); SB(); };
#pragma unroll
        for (int s = 0; s < 28; ++s) {
            if (s == GD) set_up();
            if (s == 0) __builtin_amdgcn_s_setprio(3);
            if (s == 8) __builtin_amdgcn_s_setprio(2);
            if (s == 16) __builtin_amdgcn_s_setprio(1);
            if (s == 24) __builtin_amdgcn_s_setprio(0);
            if (s >= GD) add(s - GD);
            gather(s);
        }
        if (wave < OVF_W) {
#pragma unroll
            for (int s = 28; s < 32 + GD; ++s) {
                add(s - GD);
                if (s < 32) gather(s);
            }
        } else {
            const u32x2 go = sbuf_load_b64(rm, rec_row(ov[0], ov[1], bw), 0, 0, 0);
            SB();
#pragma unroll
            for (int s = 28; s < 28 + GD; ++s) add(s - GD);
            accum_fix(wt, mg, mu, ov[0], go);
        }
        lds_barrier();
        finalize(true);
        lds_barrier();
        fast = !__builtin_amdgcn_readfirstlane(*LP(unsigned, C_FAIL));
    } else set_up();
    if (!fast) {
#pragma unroll
        for (int k = 0; k < 4; ++k) if (k * NT + tid < NLOC * 2) *LP(u32x4, L2_ACD + (k * NT + tid) * 16) = (u32x4){0, 0, 0, 0};
        lds_barrier();
        unsigned todo = 0xffffu;
        u32x3 rec = {0, 0, 0}, cu = {0, 0, 0}; u32x2 g0 = {0, 0}, g1 = {0, 0};
        int drain = 2;
#pragma unroll 1
        while (true) {
            if (rec_ok(cu[1])) accum(x, rec_la(cu[0]), rec_ai(cu[0], cu[1]), g0);
            if (rec_ok(cu[1] >> 16)) accum(x, rec_la(cu[2]), rec_ai(cu[2], cu[1] >> 16), g1);
            SB();
            if (!todo && --drain == 0) break;
            g0 = sbuf_load_b64(rm, rec_row(rec[0], rec[1], bw), 0, 0, 0);
            g1 = sbuf_load_b64(rm, rec_row(rec[2], rec[1] >> 16, bw), 0, 0, 0);
            cu = rec;
            asm volatile("" : "+v"(cu));
            SB();
            rec = fetch(todo, sg0, sg1, recs, lane * 12);
            SB();
        }
        lds_barrier();
        finalize(false);
    }
    asm volatile(".Lgem_end:");
}

__global__ void k_plain_trip(const u16* __restrict__ m, const int* __restrict__ idx_in, const int* __restrict__ idx_out,
                             const u16* __restrict__ dist, const u16* __restrict__ angle, const u16* __restrict__ centers, const u16* __restrict__ freq,
                             const u16* __restrict__ w_sbf, const u16* __restrict__ w_down, float* __restrict__ agg, int T) {
    const int t = blockIdx.x * blockDim.x + threadIdx.x;
    if (t >= T) return;
    const int i = idx_in[t], o = idx_out[t];
    const float a = bf2f(angle[t]), d = bf2f(dist[o]);
    float c[3], s[4] = {0.f, 0.f, 0.f, 0.f};
#pragma unroll
    for (int l = 0; l < 3; ++l) c[l] = cosr(bf2f(freq[l]), a);
#pragma unroll
    for (int r = 0; r < 8; ++r) {
        const float df = rbf16(d - bf2f(centers[r]));
        const float rb = rbf16(__expf(rbf16(-10.f * rbf16(df * df))));
#pragma unroll
        for (int l = 0; l < 3; ++l)
#pragma unroll
            for (int k = 0; k < 4; ++k) s[k] += rb * c[l] * bf2f(w_sbf[(r * 3 + l) * 4 + k]);
    }
#pragma unroll
    for (int k = 0; k < 4; ++k) {
        float md = 0.f;
#pragma unroll
        for (int j = 0; j < 4; ++j) md += bf2f(m[(size_t)i * 4 + j]) * bf2f(w_down[j * 4 + k]);
        atomicAdd(&agg[(size_t)o * 4 + k], md * s[k]);
    }
}
__global__ void k_plain_fin(const u16* __restrict__ m, const u16* __restrict__ dist, const u16* __restrict__ centers, const u16* __restrict__ w_rbf,
                            const u16* __restrict__ w_up, const float* __restrict__ agg, u16* __restrict__ out, int E) {
    const int e = blockIdx.x * blockDim.x + threadIdx.x;
    if (e >= E) return;
    const float d = bf2f(dist[e]);
    float g[4] = {0.f, 0.f, 0.f, 0.f};
#pragma unroll
    for (int r = 0; r < 8; ++r) {
        const float df = rbf16(d - bf2f(centers[r]));
        const float rb = rbf16(__expf(rbf16(-10.f * rbf16(df * df))));
#pragma unroll
        for (int k = 0; k < 4; ++k) g[k] += rb * bf2f(w_rbf[r * 4 + k]);
    }
#pragma unroll
    for (int k = 0; k < 4; ++k) {
        float u = 0.f;
#pragma unroll
        for (int j = 0; j < 4; ++j) u += agg[(size_t)e * 4 + j] * bf2f(w_up[j * 4 + k]);
        out[(size_t)e * 4 + k] = (u16)f2bf(bf2f(m[(size_t)e * 4 + k]) + u * g[k]);
    }
}

extern "C" void kernel_launch(void* const* d_in, const int* in_sizes, int n_in,
                              void* d_out, int out_size, void* d_ws, size_t ws_size,
                              hipStream_t stream) {
    const u16* m = (const u16*)d_in[0];
    const int* idx_in = (const int*)d_in[1];
    const int* idx_out = (const int*)d_in[2];
    const u16* dist = (const u16*)d_in[3];
    const u16* angle = (const u16*)d_in[4];
    const u16* centers = (const u16*)d_in[5];
    const u16* freq = (const u16*)d_in[6];
    const u16* w_rbf = (const u16*)d_in[7];
    const u16* w_sbf = (const u16*)d_in[8];
    const u16* w_down = (const u16*)d_in[9];
    const u16* w_up = (const u16*)d_in[10];
    const int E = in_sizes[3];
    const int T = in_sizes[1];
    const int chunk = ((T + NWG - 1) / NWG + 7) / 8 * 8;
    const int rec_stride = (((chunk + 256) / 2 * 3) + 31) / 32 * 32;
    const size_t rec_off = 32768 + (size_t)NWG * NWG * 8;
    int dev = 0, cus = 0;
    (void)hipGetDevice(&dev);
    (void)hipDeviceGetAttribute(&cus, hipDeviceAttributeMultiprocessorCount, dev);
    char* ws = (char*)d_ws;
    if (E < 1 || E > NLOC * NWG || (T & 3) || chunk > CHUNK_MAX || cus < NWG || rec_off + (size_t)NWG * rec_stride * 4 > ws_size) {
        (void)hipMemsetAsync(ws, 0, (size_t)E * 16, stream);
        if (T > 0) k_plain_trip<<<(T + 255) / 256, 256, 0, stream>>>(m, idx_in, idx_out, dist, angle, centers, freq, w_sbf, w_down, (float*)ws, T);
        if (E > 0) k_plain_fin<<<(E + 255) / 256, 256, 0, stream>>>(m, dist, centers, w_rbf, w_up, (const float*)ws, (u16*)d_out, E);
        return;
    }
    int bw = ((E + NBMAX - 1) / NBMAX + 15) / 16 * 16;
    if (bw < 272) bw = 272;
    const unsigned bmul = (unsigned)((0x100000000ull + bw - 1) / bw);
    u64* tagword = (u64*)ws;
    u64* rdy = (u64*)(ws + 4096);
    u64* tab = (u64*)(ws + 32768);
    unsigned* recs = (unsigned*)(ws + rec_off);
    (void)hipFuncSetAttribute((const void*)k_fused, hipFuncAttributeMaxDynamicSharedMemorySize, LDS_BYTES);
    k_fused<<<NWG, NT, LDS_BYTES, stream>>>(idx_in, idx_out, angle, m, dist, centers, freq, w_rbf, w_sbf, w_down, w_up, (u16*)d_out, recs, tab, rdy, tagword, T, E, chunk, rec_stride, bmul, bw);
}
```
